```python
import torch

def _softmax(x, axis=-1):
    return torch.softmax(x, dim=axis)
def _gelu(x):
    return torch.nn.functional.gelu(x, approximate="tanh")
def _silu(x):
    return torch.nn.functional.silu(x)
def _sigmoid(x):
    return torch.sigmoid(x)
def _layernorm(x, axis=-1, eps=1e-5):
    m = x.mean(dim=axis, keepdim=True)
    v = x.var(dim=axis, keepdim=True, unbiased=False)
    return (x - m) / torch.sqrt(v + eps)
def _scatter_add(tgt, idx, src, axis=0):
    return tgt.clone().index_add_(axis, idx, src)
def _segment_sum(src, seg, n):
    out = torch.zeros((n,) + src.shape[1:], dtype=src.dtype)
    return out.index_add_(0, seg, src)
def _pad(x, pw):
    flat = tuple(v for pair in reversed(pw) for v in pair)
    return torch.nn.functional.pad(x, flat)

kernel_name = 'standalone__sliding_block_cached__decode'

def setup_inputs():

    _w = (lambda a: (lambda t: t.to(torch.bfloat16) if t.is_floating_point() else t)(torch.as_tensor(a)))
    B, Sq, D = 1, 1, 640
    Hq, Hkv, Sk, Dh = 32, 32, 16384, 128
    assert Hq % Hkv == 0, (Hq, Hkv)
    return {
        "x": _w((lambda *s: torch.randn(*s, dtype=torch.bfloat16) * 0.8)(B, Sq, D)),
        "k_cache": _w((lambda *s: torch.randn(*s, dtype=torch.bfloat16) * 0.8)(B, Hkv, Sk, Dh)),
        "v_cache": _w((lambda *s: torch.randn(*s, dtype=torch.bfloat16) * 0.05)(B, Hkv, Sk, Dh)),
        "wq": _w((lambda *s: torch.randn(*s, dtype=torch.bfloat16) * 0.8)(D, Hq * Dh) * (D ** -0.5)),
        "wo": _w((lambda *s: torch.randn(*s, dtype=torch.bfloat16) * 0.05)(Hq * Dh, D) * ((Hq * Dh) ** -0.5)),
    }

def reference(x, k_cache, v_cache, wq, wo):
    B, Sq, D = x.shape
    Hq, Dh = 32, 128
    Hkv, Sk = k_cache.shape[1], k_cache.shape[2]
    W = min(4096, max(256, Sk // 4))
    rep = Hq // Hkv
    q = (lambda x, *d: x.permute(*d))((x @ wq).reshape(B, Sq, Hq, Dh), 0, 2, 1, 3)
    k = (lambda x, n, axis: torch.repeat_interleave(x, n, dim=axis))(k_cache[:, :, Sk - W:, :], rep, axis=1)
    v = (lambda x, n, axis: torch.repeat_interleave(x, n, dim=axis))(v_cache[:, :, Sk - W:, :], rep, axis=1)
    scores = torch.einsum("bhqd,bhkd->bhqk", q, k) * (Dh ** -0.5)
    p = _softmax(scores, axis=-1)
    o = torch.einsum("bhqk,bhkd->bhqd", p, v)
    return (lambda x, *d: x.permute(*d))(o, 0, 2, 1, 3).reshape(B, Sq, Hq * Dh) @ wo
```

```cpp
#include <hip/hip_runtime.h>
#include <hip/hip_bf16.h>
#include <stdint.h>

#define GAS __attribute__((address_space(1)))
#define RLX_AGENT __ATOMIC_RELAXED, __HIP_MEMORY_SCOPE_AGENT
typedef unsigned v4u __attribute__((ext_vector_type(4)));
typedef unsigned v2u __attribute__((ext_vector_type(2)));
typedef float f32x4 __attribute__((ext_vector_type(4)));
typedef float f32x2 __attribute__((ext_vector_type(2)));
typedef __bf16 bf16x2 __attribute__((ext_vector_type(2)));
typedef __bf16 bf16x8 __attribute__((ext_vector_type(8)));
typedef GAS unsigned gu32;
typedef GAS unsigned long long gu64;
typedef GAS v4u gv4u;

namespace {
constexpr int DM = 640, NH = 32, DH = 128, SK = 16384, WIN = 4096;
constexpr int NSPLIT = 8, KPW = WIN / NSPLIT;
constexpr int NQ = NH * DH;
constexpr int COLS = DM / NSPLIT;
constexpr int WS_EPOCH = 0;
constexpr int WS_ML = 4096;
constexpr int WS_P1 = 8192;
constexpr int WS_P2 = WS_P1 + NH * NSPLIT * 512;
constexpr int WS_BYTES = WS_P2 + 80 * 2048;
constexpr unsigned SPIN_CAP = 1u << 16;
constexpr float C2 = 0.08838834764831845f * 1.4426950408889634f;

__device__ __forceinline__ float dot2(unsigned a, unsigned b, float acc) {
  return __builtin_amdgcn_fdot2_f32_bf16(__builtin_bit_cast(bf16x2, a), __builtin_bit_cast(bf16x2, b), acc, false);
}
__device__ __forceinline__ unsigned cvtpk(float lo, float hi) {
  bf16x2 v = {(__bf16)lo, (__bf16)hi};
  return __builtin_bit_cast(unsigned, v);
}
template <int CTRL> __device__ __forceinline__ float dppf(float x) {
  return __builtin_bit_cast(float, __builtin_amdgcn_mov_dpp(__builtin_bit_cast(int, x), CTRL, 0xf, 0xf, true));
}
template <int CTRL> __device__ __forceinline__ unsigned dppu(unsigned x) {
  return (unsigned)__builtin_amdgcn_mov_dpp((int)x, CTRL, 0xf, 0xf, true);
}
constexpr int XOR1 = 0xB1, XOR2 = 0x4E, XOR7 = 0x141, ROR8 = 0x128, ROR4 = 0x124, BCAST0 = 0x150;
__device__ __forceinline__ unsigned perm_lo(unsigned a, unsigned b) { return __builtin_amdgcn_perm(b, a, 0x05040100u); }
__device__ __forceinline__ unsigned perm_hi(unsigned a, unsigned b) { return __builtin_amdgcn_perm(b, a, 0x07060302u); }
__device__ __forceinline__ float swap32_sum(float a, float b) {
  auto t = __builtin_amdgcn_permlane32_swap(__float_as_uint(a), __float_as_uint(b), false, false);
  return __uint_as_float(t[0]) + __uint_as_float(t[1]);
}
__device__ __forceinline__ float swap16_sum(float a, float b) {
  auto t = __builtin_amdgcn_permlane16_swap(__float_as_uint(a), __float_as_uint(b), false, false);
  return __uint_as_float(t[0]) + __uint_as_float(t[1]);
}
__device__ __forceinline__ float xrow_max(float x) {
  auto s = __builtin_amdgcn_permlane16_swap(__float_as_uint(x), __float_as_uint(x), false, false);
  x = fmaxf(__uint_as_float(s[0]), __uint_as_float(s[1]));
  auto t = __builtin_amdgcn_permlane32_swap(__float_as_uint(x), __float_as_uint(x), false, false);
  return fmaxf(__uint_as_float(t[0]), __uint_as_float(t[1]));
}
__device__ __forceinline__ float wave_max(float x) {
  x = fmaxf(x, dppf<XOR1>(x));
  x = fmaxf(x, dppf<XOR2>(x));
  x = fmaxf(x, dppf<ROR4>(x));
  x = fmaxf(x, dppf<ROR8>(x));
  return xrow_max(x);
}
__device__ __forceinline__ float wave_sum(float x) {
  x += dppf<XOR1>(x);
  x += dppf<XOR2>(x);
  x += dppf<ROR4>(x);
  x += dppf<ROR8>(x);
  x = swap16_sum(x, x);
  return swap32_sum(x, x);
}
__device__ __forceinline__ f32x2 rows_reduce8(const float (&a)[8]) {
  const float u0 = swap32_sum(a[0], a[4]), u1 = swap32_sum(a[1], a[5]), u2 = swap32_sum(a[2], a[6]), u3 = swap32_sum(a[3], a[7]);
  f32x2 t;
  t.x = swap16_sum(u0, u2);
  t.y = swap16_sum(u1, u3);
  return t;
}
__device__ __forceinline__ v4u ld_nt(const gv4u* p) { return __builtin_nontemporal_load(p); }
__device__ __forceinline__ v4u ld_sc1(__amdgpu_buffer_rsrc_t rs, int off) { return __builtin_amdgcn_raw_buffer_load_b128(rs, off, 0, 16); }
__device__ __forceinline__ void st_granule(GAS unsigned char* ws, int off, unsigned val, unsigned tag) {
  __hip_atomic_store((gu64*)(ws + off), ((unsigned long long)tag << 32) | val, RLX_AGENT);
}
#define SCHED_FENCE() __builtin_amdgcn_sched_barrier(0)
#define MEM_FENCE() asm volatile("" ::: "memory")

__global__ void __launch_bounds__(512, 2)
fused_decode(const unsigned short* __restrict__ x_, const unsigned short* __restrict__ kc_, const unsigned short* __restrict__ vc_,
             const unsigned short* __restrict__ wq_, const unsigned short* __restrict__ wo_, unsigned short* __restrict__ out_,
             unsigned char* __restrict__ ws_) {
  __shared__ __attribute__((aligned(16))) f32x2 lds_q[8 * 64];
  __shared__ __attribute__((aligned(16))) f32x2 lds_o[8 * 64];
  __shared__ __attribute__((aligned(16))) f32x2 lds_ml[8];
  const int b = blockIdx.x, h = b & 31, s = b >> 5;
  const int tid = threadIdx.x, lane = tid & 63, w = __builtin_amdgcn_readfirstlane(tid >> 6), r = lane >> 4, c = lane & 15;
  GAS unsigned char* ws = (GAS unsigned char*)ws_;

  const unsigned E = __hip_atomic_load((gu32*)(ws + WS_EPOCH), RLX_AGENT);
  unsigned xp[10];
  {
    const GAS unsigned* x32 = (const GAS unsigned*)x_ + 40 * w + 10 * r;
#pragma unroll
    for (int t = 0; t < 5; ++t) {
      const v2u v = *(const GAS v2u*)(x32 + 2 * t);
      xp[2 * t] = v.x;
      xp[2 * t + 1] = v.y;
    }
  }
  v4u wqv[20];
  {
    const GAS unsigned short* p = (const GAS unsigned short*)wq_ + (size_t)(80 * w + 20 * r) * NQ + h * DH + 8 * c;
#pragma unroll
    for (int i = 0; i < 20; ++i) wqv[i] = *(const gv4u*)(p + (size_t)i * NQ);
  }
  SCHED_FENCE();
  const size_t kvoff = ((size_t)h * SK + (SK - WIN) + KPW * s + 64 * w + r) * DH + 8 * c;
  v4u kv[16], vv[16];
  {
    const GAS unsigned short* p = (const GAS unsigned short*)kc_ + kvoff;
#pragma unroll
    for (int i = 0; i < 16; ++i) kv[i] = ld_nt((const gv4u*)(p + i * 4 * DH));
  }
  SCHED_FENCE();
  const GAS unsigned short* vp = (const GAS unsigned short*)vc_ + kvoff;
#pragma unroll
  for (int i = 0; i < 8; ++i) vv[i] = ld_nt((const gv4u*)(vp + i * 4 * DH));
  SCHED_FENCE();

  {
    float a[8];
#pragma unroll
    for (int k = 0; k < 8; ++k) a[k] = 0.f;
#pragma unroll
    for (int t = 0; t < 10; ++t) {
#pragma unroll
      for (int k = 0; k < 4; ++k) {
        a[2 * k] = dot2(perm_lo(wqv[2 * t][k], wqv[2 * t + 1][k]), xp[t], a[2 * k]);
        a[2 * k + 1] = dot2(perm_hi(wqv[2 * t][k], wqv[2 * t + 1][k]), xp[t], a[2 * k + 1]);
      }
    }
    lds_q[w * 64 + 4 * c + r] = rows_reduce8(a);
  }
  SCHED_FENCE();
#pragma unroll
  for (int i = 8; i < 16; ++i) vv[i] = ld_nt((const gv4u*)(vp + i * 4 * DH));
  const unsigned tag = E + 1u;
  const int p1_off = WS_P1 + (h * NSPLIT + s) * 512 + lane * 8;
  const int nt = w < 1 ? 0 : (w > 5 ? 4 : w - 1);
  const int col = s * COLS + nt * 16 + c;
  const int p2_off = WS_P2 + (col >> 3) * 2048 + h * 64 + (col & 7) * 8;
  if (w == 0) st_granule(ws, p1_off, 0u, E);
  else if (w <= 5 && lane < 16) st_granule(ws, p2_off, 0u, E);
  SCHED_FENCE();
  __syncthreads();
  unsigned q[4];
  {
    f32x2 t = lds_q[4 * c + r];
#pragma unroll
    for (int ww = 1; ww < 8; ++ww) t += lds_q[ww * 64 + 4 * c + r];
    const unsigned qd = cvtpk(t.x, t.y);
    auto s1 = __builtin_amdgcn_permlane16_swap(qd, qd, false, false);
    auto s2 = __builtin_amdgcn_permlane32_swap(s1[0], s1[0], false, false);
    auto s3 = __builtin_amdgcn_permlane32_swap(s1[1], s1[1], false, false);
    q[0] = s2[0];
    q[1] = s3[0];
    q[2] = s2[1];
    q[3] = s3[1];
  }

  float sc;
  {
    float xs[16];
#pragma unroll
    for (int i = 0; i < 16; ++i) {
      float d = 0.f;
#pragma unroll
      for (int k = 0; k < 4; ++k) d = dot2(kv[i][k], q[k], d);
      xs[i] = d;
    }
#define TR_STEP(HALF, CTRL, BIT)                                   \
  _Pragma("unroll") for (int j = 0; j < (HALF); ++j) {             \
    const float lo_ = xs[j], hi_ = xs[j + (HALF)];                 \
    const float t1_ = lo_ + dppf<CTRL>(lo_), t2_ = hi_ + dppf<CTRL>(hi_); \
    xs[j] = (c & (BIT)) ? t2_ : t1_;                               \
  }
    TR_STEP(8, ROR8, 8) TR_STEP(4, XOR7, 4) TR_STEP(2, XOR2, 2) TR_STEP(1, XOR1, 1)
#undef TR_STEP
    sc = xs[0] * C2;
  }
  unsigned short braw[32];
  {
    const GAS unsigned short* p = (const GAS unsigned short*)wo_ + (size_t)(h * DH + 8 * r) * DM + col;
#pragma unroll
    for (int ks = 0; ks < 4; ++ks)
#pragma unroll
      for (int j = 0; j < 8; ++j) braw[8 * ks + j] = p[(size_t)(32 * ks + j) * DM];
  }
  SCHED_FENCE();
  const float mw = wave_max(sc);
  float p = __builtin_amdgcn_exp2f(sc - mw);
  const float lw = wave_sum(p);
  if (lane == 0) lds_ml[w] = f32x2{mw, lw};
  __syncthreads();
  {
    float M = lds_ml[0].x;
#pragma unroll
    for (int ww = 1; ww < 8; ++ww) M = fmaxf(M, lds_ml[ww].x);
    float L = 0.f;
#pragma unroll
    for (int ww = 0; ww < 8; ++ww) L += lds_ml[ww].y * __builtin_amdgcn_exp2f(lds_ml[ww].x - M);
    p *= __builtin_amdgcn_exp2f(mw - M) / L;
    if (w == 0 && lane < 2) st_granule(ws, WS_ML + (h * NSPLIT + s) * 16 + lane * 8, __float_as_uint(lane == 0 ? M : L), tag);
  }
  const unsigned ppk = cvtpk(p, dppf<XOR1>(p));

  {
    float a[8];
#pragma unroll
    for (int k = 0; k < 8; ++k) a[k] = 0.f;
#define PV_STEP(J)                                                                    \
  {                                                                                   \
    const unsigned pp = dppu<BCAST0 + 2 * (J)>(ppk);                                  \
    _Pragma("unroll") for (int k = 0; k < 4; ++k) {                                   \
      a[2 * k] = dot2(perm_lo(vv[2 * (J)][k], vv[2 * (J) + 1][k]), pp, a[2 * k]);         \
      a[2 * k + 1] = dot2(perm_hi(vv[2 * (J)][k], vv[2 * (J) + 1][k]), pp, a[2 * k + 1]); \
    }                                                                                 \
  }
    PV_STEP(0) PV_STEP(1) PV_STEP(2) PV_STEP(3) PV_STEP(4) PV_STEP(5) PV_STEP(6) PV_STEP(7)
#undef PV_STEP
    lds_o[w * 64 + 4 * c + r] = rows_reduce8(a);
  }
  __syncthreads();

  const __amdgpu_buffer_rsrc_t rs = __builtin_amdgcn_make_buffer_rsrc((void*)ws_, 0, WS_BYTES, 0x00020000);
  if (w == 0) {
    f32x2 t = lds_o[lane];
#pragma unroll
    for (int ww = 1; ww < 8; ++ww) t += lds_o[ww * 64 + lane];
    st_granule(ws, p1_off, cvtpk(t.x, t.y), tag);
  } else if (w <= 5) {
    float cf[4];
    {
      const int off = WS_ML + (h * NSPLIT + 4 * (r & 1)) * 16;
      v4u ml[4];
      for (unsigned spins = 0;; ++spins) {
        MEM_FENCE();
#pragma unroll
        for (int j = 0; j < 4; ++j) ml[j] = ld_sc1(rs, off + 16 * j);
        bool ok = true;
#pragma unroll
        for (int j = 0; j < 4; ++j) ok &= (ml[j].y == tag) & (ml[j].w == tag);
        if (__all(ok) || spins > SPIN_CAP) break;
        __builtin_amdgcn_s_sleep(1);
      }
      float M = fmaxf(fmaxf(__uint_as_float(ml[0].x), __uint_as_float(ml[1].x)), fmaxf(__uint_as_float(ml[2].x), __uint_as_float(ml[3].x)));
      M = xrow_max(M);
#pragma unroll
      for (int j = 0; j < 4; ++j) cf[j] = __uint_as_float(ml[j].z) * __builtin_amdgcn_exp2f(__uint_as_float(ml[j].x) - M);
      const float ls = (cf[0] + cf[1]) + (cf[2] + cf[3]);
      const float inv = 1.0f / swap16_sum(ls, ls);
#pragma unroll
      for (int j = 0; j < 4; ++j) cf[j] *= inv;
    }
    v4u g[8];
    {
      const int off = WS_P1 + (h * NSPLIT + (lane & 7)) * 512 + r * 32;
      for (unsigned spins = 0;; ++spins) {
        MEM_FENCE();
#pragma unroll
        for (int ks = 0; ks < 4; ++ks) {
          g[2 * ks] = ld_sc1(rs, off + ks * 128);
          g[2 * ks + 1] = ld_sc1(rs, off + ks * 128 + 16);
        }
        bool ok = true;
#pragma unroll
        for (int i = 0; i < 8; ++i) ok &= (g[i].y == tag) & (g[i].w == tag);
        if (__all(ok) || spins > SPIN_CAP) break;
        __builtin_amdgcn_s_sleep(1);
      }
    }
    f32x4 acc = {0.f, 0.f, 0.f, 0.f};
#pragma unroll
    for (int ks = 0; ks < 4; ++ks) {
      const v4u af = {g[2 * ks].x, g[2 * ks].z, g[2 * ks + 1].x, g[2 * ks + 1].z};
      v4u bf;
#pragma unroll
      for (int j = 0; j < 4; ++j) bf[j] = (unsigned)braw[8 * ks + 2 * j] | ((unsigned)braw[8 * ks + 2 * j + 1] << 16);
      acc = __builtin_amdgcn_mfma_f32_16x16x32_bf16(__builtin_bit_cast(bf16x8, af), __builtin_bit_cast(bf16x8, bf), acc, 0, 0, 0);
    }
    float y = (cf[0] * acc[0] + cf[1] * acc[1]) + (cf[2] * acc[2] + cf[3] * acc[3]);
    y = swap16_sum(y, y);
    if (lane < 16) st_granule(ws, p2_off, __float_as_uint(y), tag);
  } else if (w == 6 && h < 10) {
    const int cg = s * 10 + h;
    const int off = WS_P2 + cg * 2048 + lane * 32;
    v4u g0, g1;
    for (unsigned spins = 0;; ++spins) {
      MEM_FENCE();
      g0 = ld_sc1(rs, off);
      g1 = ld_sc1(rs, off + 16);
      const bool ok = (g0.y == tag) & (g0.w == tag) & (g1.y == tag) & (g1.w == tag);
      if (__all(ok) || spins > SPIN_CAP) break;
      __builtin_amdgcn_s_sleep(1);
    }
    float v[4] = {__uint_as_float(g0.x), __uint_as_float(g0.z), __uint_as_float(g1.x), __uint_as_float(g1.z)};
#pragma unroll
    for (int j = 0; j < 4; ++j) {
      float t = v[j];
      t += dppf<XOR2>(t);
      t += dppf<ROR4>(t);
      t += dppf<ROR8>(t);
      t = swap16_sum(t, t);
      v[j] = swap32_sum(t, t);
    }
    if (lane < 2) {
      v2u o = {cvtpk(v[0], v[1]), cvtpk(v[2], v[3])};
      *(GAS v2u*)((GAS unsigned short*)out_ + cg * 8 + lane * 4) = o;
    }
    if (b == 0 && lane == 0) __hip_atomic_store((gu32*)(ws + WS_EPOCH), tag, RLX_AGENT);
  }
}
}

extern "C" void kernel_launch(void* const* d_in, const int* in_sizes, int n_in, void* d_out, int out_size, void* d_ws, size_t ws_size,
                              hipStream_t stream) {
  (void)in_sizes; (void)n_in; (void)out_size; (void)ws_size;
  fused_decode<<<NH * NSPLIT, 512, 0, stream>>>((const unsigned short*)d_in[0], (const unsigned short*)d_in[1], (const unsigned short*)d_in[2],
                                                (const unsigned short*)d_in[3], (const unsigned short*)d_in[4], (unsigned short*)d_out,
                                                (unsigned char*)d_ws);
}
```

```cpp
#include <hip/hip_runtime.h>
#include <hip/hip_bf16.h>
#include <stdint.h>

#define GAS __attribute__((address_space(1)))
#define RLX_AGENT __ATOMIC_RELAXED, __HIP_MEMORY_SCOPE_AGENT
typedef unsigned v4u __attribute__((ext_vector_type(4)));
typedef unsigned v2u __attribute__((ext_vector_type(2)));
typedef float f32x4 __attribute__((ext_vector_type(4)));
typedef float f32x2 __attribute__((ext_vector_type(2)));
typedef __bf16 bf16x2 __attribute__((ext_vector_type(2)));
typedef GAS unsigned gu32;
typedef GAS unsigned long long gu64;

namespace {
constexpr int DM = 640, NH = 32, DH = 128, SK = 16384, WIN = 4096;
constexpr int NSPLIT = 8;
constexpr int NQ = NH * DH;
constexpr int WS_EPOCH = 0;
constexpr int WS_LSE = 4096;
constexpr int WS_Q0 = WS_LSE + NH * NSPLIT * 128;
constexpr int WS_P1 = WS_Q0 + NH * NSPLIT * 1024;
constexpr int WS_P2 = WS_P1 + NH * NSPLIT * 1024;
constexpr int WS_BYTES = WS_P2 + 80 * 256 * 64;
constexpr unsigned SPIN_CAP = 1u << 16;
constexpr float C2 = 0.08838834764831845f * 1.4426950408889634f;
constexpr int AUX_NT = 2, AUX_SC1 = 16;

__device__ __forceinline__ float dot2(unsigned a, unsigned b, float acc) {
  return __builtin_amdgcn_fdot2_f32_bf16(__builtin_bit_cast(bf16x2, a), __builtin_bit_cast(bf16x2, b), acc, false);
}
__device__ __forceinline__ unsigned cvtpk(float lo, float hi) {
  bf16x2 v = {(__bf16)lo, (__bf16)hi};
  return __builtin_bit_cast(unsigned, v);
}
template <int CTRL> __device__ __forceinline__ float dppf(float x) {
  return __builtin_bit_cast(float, __builtin_amdgcn_mov_dpp(__builtin_bit_cast(int, x), CTRL, 0xf, 0xf, true));
}
template <int CTRL> __device__ __forceinline__ unsigned dppu(unsigned x) {
  return (unsigned)__builtin_amdgcn_mov_dpp((int)x, CTRL, 0xf, 0xf, true);
}
constexpr int XOR1 = 0xB1, XOR2 = 0x4E, XOR7 = 0x141, ROR8 = 0x128, ROR4 = 0x124, BCAST0 = 0x150;
__device__ __forceinline__ unsigned perm_lo(unsigned a, unsigned b) { return __builtin_amdgcn_perm(b, a, 0x05040100u); }
__device__ __forceinline__ unsigned perm_hi(unsigned a, unsigned b) { return __builtin_amdgcn_perm(b, a, 0x07060302u); }
__device__ __forceinline__ float swap32_sum(float a, float b) {
  auto t = __builtin_amdgcn_permlane32_swap(__float_as_uint(a), __float_as_uint(b), false, false);
  return __uint_as_float(t[0]) + __uint_as_float(t[1]);
}
__device__ __forceinline__ float swap16_sum(float a, float b) {
  auto t = __builtin_amdgcn_permlane16_swap(__float_as_uint(a), __float_as_uint(b), false, false);
  return __uint_as_float(t[0]) + __uint_as_float(t[1]);
}
__device__ __forceinline__ float xrow_max(float x) {
  auto s = __builtin_amdgcn_permlane16_swap(__float_as_uint(x), __float_as_uint(x), false, false);
  x = fmaxf(__uint_as_float(s[0]), __uint_as_float(s[1]));
  auto t = __builtin_amdgcn_permlane32_swap(__float_as_uint(x), __float_as_uint(x), false, false);
  return fmaxf(__uint_as_float(t[0]), __uint_as_float(t[1]));
}
__device__ __forceinline__ float xrow_sum(float x) {
  x = swap16_sum(x, x);
  return swap32_sum(x, x);
}
__device__ __forceinline__ float wave_max(float x) {
  x = fmaxf(x, dppf<XOR1>(x));
  x = fmaxf(x, dppf<XOR2>(x));
  x = fmaxf(x, dppf<ROR4>(x));
  x = fmaxf(x, dppf<ROR8>(x));
  return xrow_max(x);
}
__device__ __forceinline__ float wave_sum(float x) {
  x += dppf<XOR1>(x);
  x += dppf<XOR2>(x);
  x += dppf<ROR4>(x);
  x += dppf<ROR8>(x);
  return xrow_sum(x);
}
__device__ __forceinline__ f32x2 rows_reduce8(const float (&a)[8]) {
  const float u0 = swap32_sum(a[0], a[4]), u1 = swap32_sum(a[1], a[5]), u2 = swap32_sum(a[2], a[6]), u3 = swap32_sum(a[3], a[7]);
  f32x2 t;
  t.x = swap16_sum(u0, u2);
  t.y = swap16_sum(u1, u3);
  return t;
}
typedef __amdgpu_buffer_rsrc_t rsrc_t;
__device__ __forceinline__ rsrc_t mk_rsrc(const void* p, int bytes) { return __builtin_amdgcn_make_buffer_rsrc((void*)p, 0, bytes, 0x00020000); }
__device__ __forceinline__ v4u ld16_sc1(rsrc_t rs, int off) { return __builtin_amdgcn_raw_buffer_load_b128(rs, off, 0, AUX_SC1); }
__device__ __forceinline__ v2u ld8_sc1(rsrc_t rs, int off) { return __builtin_amdgcn_raw_buffer_load_b64(rs, off, 0, AUX_SC1); }
__device__ __forceinline__ void st16_sc1(rsrc_t rs, int off, float a, float b, unsigned tag) {
  __builtin_amdgcn_raw_buffer_store_b128(v4u{__float_as_uint(a), tag, __float_as_uint(b), tag}, rs, off, 0, AUX_SC1);
}
__device__ __forceinline__ void st8_sc1(rsrc_t rs, int off, float a, unsigned tag) {
  __builtin_amdgcn_raw_buffer_store_b64(v2u{__float_as_uint(a), tag}, rs, off, 0, AUX_SC1);
}
#define SCHED_FENCE() __builtin_amdgcn_sched_barrier(0)
#define MEM_FENCE() asm volatile("" ::: "memory")
#define STAMP(k)

#define WIN_W 3
#define THR_N 24
template <int N> __device__ __forceinline__ void waitvm() { asm volatile("s_waitcnt vmcnt(%0)" ::"n"(N) : "memory"); }
#define LAS __attribute__((address_space(3)))
__device__ __forceinline__ unsigned lds_ld(const unsigned* p) { return __hip_atomic_load((const LAS unsigned*)p, __ATOMIC_RELAXED, __HIP_MEMORY_SCOPE_WORKGROUP); }
__device__ __forceinline__ void lds_st(unsigned* p, unsigned v) { __hip_atomic_store((LAS unsigned*)p, v, __ATOMIC_RELAXED, __HIP_MEMORY_SCOPE_WORKGROUP); }
#define LGKM0() asm volatile("s_waitcnt lgkmcnt(0)" ::: "memory")

__global__ void __launch_bounds__(576, 3)
fused_decode(const unsigned short* __restrict__ x_, const unsigned short* __restrict__ kc_, const unsigned short* __restrict__ vc_,
             const unsigned short* __restrict__ wq_, const unsigned short* __restrict__ wo_, unsigned short* __restrict__ out_,
             unsigned char* __restrict__ ws_) {
  __shared__ __attribute__((aligned(16))) f32x2 lds_o[8 * 64];
  __shared__ __attribute__((aligned(16))) f32x2 lds_ml[8];
  __shared__ __attribute__((aligned(16))) unsigned lds_q[64];
  __shared__ __attribute__((aligned(16))) unsigned lds_of[8];
  __shared__ __attribute__((aligned(16))) unsigned lds_ctl[4];
  const int b = blockIdx.x, h = b & 31, s = b >> 5;
  const int tid = threadIdx.x, lane = tid & 63, w = __builtin_amdgcn_readfirstlane(tid >> 6), r = lane >> 4, c = lane & 15;
  STAMP(0);
  asm volatile("" ::"s"(x_), "s"(kc_), "s"(vc_), "s"(wq_), "s"(wo_), "s"(out_), "s"(ws_));
  const rsrc_t rs = mk_rsrc(ws_, WS_BYTES);

  if (__builtin_expect(w == 0, 1)) {
    __builtin_amdgcn_s_setprio(3);
    const rsrc_t rw = mk_rsrc(wq_ + (size_t)(80 * s) * NQ + h * DH, 80 * NQ * 2);
    v4u wqv[20];
#pragma unroll
    for (int i = 0; i < 20; ++i) wqv[i] = __builtin_amdgcn_raw_buffer_load_b128(rw, r * (20 * NQ * 2) + c * 16, i * (NQ * 2), AUX_NT);
    unsigned xp[10];
    {
      const GAS unsigned* x32 = (const GAS unsigned*)x_ + 40 * s + 10 * r;
#pragma unroll
      for (int t = 0; t < 5; ++t) {
        const v2u v = *(const GAS v2u*)(x32 + 2 * t);
        xp[2 * t] = v.x;
        xp[2 * t + 1] = v.y;
      }
    }
    const unsigned E = __builtin_amdgcn_raw_buffer_load_b32(rs, WS_EPOCH, 0, AUX_SC1);
    if (lane < 4) lds_st(&lds_ctl[lane], 0u);
    LGKM0();
    SCHED_FENCE();
    __builtin_amdgcn_s_barrier();
    SCHED_FENCE();
    float a[8];
#pragma unroll
    for (int k = 0; k < 8; ++k) a[k] = 0.f;
#pragma unroll
    for (int t = 0; t < 10; ++t) {
#pragma unroll
      for (int k = 0; k < 4; ++k) {
        a[2 * k] = dot2(perm_lo(wqv[2 * t][k], wqv[2 * t + 1][k]), xp[t], a[2 * k]);
        a[2 * k + 1] = dot2(perm_hi(wqv[2 * t][k], wqv[2 * t + 1][k]), xp[t], a[2 * k + 1]);
      }
    }
    const f32x2 qp = rows_reduce8(a);
    const unsigned tag = E + 1u;
    st16_sc1(rs, WS_Q0 + (h * NSPLIT + s) * 1024 + (4 * c + r) * 16, qp.x, qp.y, tag);
    STAMP(1);
    const int p1_off = WS_P1 + ((h * NSPLIT + (lane >> 3)) * NSPLIT + s) * 128 + (lane & 7) * 16;
    const int lse_off = WS_LSE + (h * NSPLIT + (lane & 7)) * 128 + s * 8;
    {
      v4u g[8];
      const int off = WS_Q0 + h * NSPLIT * 1024 + lane * 16;
      for (unsigned spins = 0;; ++spins) {
        MEM_FENCE();
#pragma unroll
        for (int i = 0; i < 8; ++i) g[i] = ld16_sc1(rs, off + i * 1024);
        bool ok = true;
#pragma unroll
        for (int i = 0; i < 8; ++i) ok &= (g[i].y == tag) & (g[i].w == tag);
        if (__all(ok) || spins > SPIN_CAP) break;
      }
      float q0 = __uint_as_float(g[0].x), q1 = __uint_as_float(g[0].z);
#pragma unroll
      for (int i = 1; i < 8; ++i) {
        q0 += __uint_as_float(g[i].x);
        q1 += __uint_as_float(g[i].z);
      }
      lds_q[lane] = cvtpk(q0, q1);
      if (lane == 0) lds_st(&lds_ctl[1], tag);
      LGKM0();
      if (lane == 0) lds_st(&lds_ctl[0], 1u);
    }
    if (lane < 8) st8_sc1(rs, lse_off, 0.f, E);
    STAMP(2);
    __syncthreads();
    STAMP(4);
    {
      float M = lds_ml[0].x;
#pragma unroll
      for (int u = 1; u < 8; ++u) M = fmaxf(M, lds_ml[u].x);
      float L = 0.f, o0 = 0.f, o1 = 0.f;
#pragma unroll
      for (int u = 0; u < 8; ++u) {
        const f32x2 ml = lds_ml[u];
        const float f = __builtin_amdgcn_exp2f(ml.x - M);
        const f32x2 ov = lds_o[u * 64 + lane];
        L = fmaf(f, ml.y, L);
        o0 = fmaf(f, ov.x, o0);
        o1 = fmaf(f, ov.y, o1);
      }
      const float inv = 1.0f / L;
      st16_sc1(rs, p1_off, o0 * inv, o1 * inv, tag);
      if (lane < 8) st8_sc1(rs, lse_off, M + __builtin_amdgcn_logf(L), tag);
    }
    STAMP(5);
    {
      v4u g;
      v2u gl;
      const int off = WS_P1 + (h * NSPLIT + s) * 1024 + lane * 16;
      const int loff = WS_LSE + (h * NSPLIT + s) * 128 + (lane >> 3) * 8;
      for (unsigned spins = 0;; ++spins) {
        MEM_FENCE();
        g = ld16_sc1(rs, off);
        gl = ld8_sc1(rs, loff);
        const bool ok = (g.y == tag) & (g.w == tag) & (gl.y == tag);
        if (__all(ok) || spins > SPIN_CAP) break;
      }
      STAMP(6);
      const float lse = __uint_as_float(gl.x);
      float mx = fmaxf(lse, dppf<ROR8>(lse));
      mx = xrow_max(mx);
      const float e = __builtin_amdgcn_exp2f(lse - mx);
      float o0 = e * __uint_as_float(g.x), o1 = e * __uint_as_float(g.z), tot = e;
      o0 += dppf<ROR8>(o0);
      o1 += dppf<ROR8>(o1);
      tot += dppf<ROR8>(tot);
      o0 = xrow_sum(o0);
      o1 = xrow_sum(o1);
      tot = xrow_sum(tot);
      const float inv = 1.0f / tot;
      if (lane < 8) lds_of[lane] = cvtpk(o0 * inv, o1 * inv);
      LGKM0();
      if (lane == 0) lds_st(&lds_ctl[2], 1u);
    }
  } else {
    const int u = w - 1;
    const size_t kvbase = ((size_t)h * SK + (SK - WIN) + (u * NSPLIT + s) * 64) * DH;
    const rsrc_t rk = mk_rsrc(kc_ + kvbase, 64 * DH * 2), rv = mk_rsrc(vc_ + kvbase, 64 * DH * 2);
    const int rot = (b * 5 + (b >> 3)) & 15;
    v4u kv[16], vv[16];
    SCHED_FENCE();
    __builtin_amdgcn_s_barrier();
    SCHED_FENCE();
#pragma unroll
    for (int i = 0; i < 16; ++i) {
      if (i >= WIN_W && i < THR_N) waitvm<WIN_W - 1>();
      kv[i] = __builtin_amdgcn_raw_buffer_load_b128(rk, lane * 16, ((i + rot) & 15) * 1024, AUX_NT);
      SCHED_FENCE();
    }
#pragma unroll
    for (int i = 0; i < 16; ++i) {
      if (i + 16 < THR_N) waitvm<WIN_W - 1>();
      vv[i] = __builtin_amdgcn_raw_buffer_load_b128(rv, lane * 16, ((i + rot) & 15) * 1024, AUX_NT);
      SCHED_FENCE();
    }
    STAMP(1);
    while (lds_ld(&lds_ctl[0]) == 0u) __builtin_amdgcn_s_sleep(2);
    MEM_FENCE();
    STAMP(2);
    const v4u q = *(const v4u*)&lds_q[4 * c];
    const unsigned tag = __builtin_amdgcn_readfirstlane(lds_ld(&lds_ctl[1]));
    float sc;
    {
      float xs[16];
#pragma unroll
      for (int i = 0; i < 16; ++i) {
        float d = 0.f;
#pragma unroll
        for (int k = 0; k < 4; ++k) d = dot2(kv[i][k], q[k], d);
        xs[i] = d;
      }
#define TR_STEP(HALF, CTRL, BIT)                                          \
  _Pragma("unroll") for (int j = 0; j < (HALF); ++j) {                    \
    const float lo_ = xs[j], hi_ = xs[j + (HALF)];                        \
    const float t1_ = lo_ + dppf<CTRL>(lo_), t2_ = hi_ + dppf<CTRL>(hi_); \
    xs[j] = (c & (BIT)) ? t2_ : t1_;                                      \
  }
      TR_STEP(8, ROR8, 8) TR_STEP(4, XOR7, 4) TR_STEP(2, XOR2, 2) TR_STEP(1, XOR1, 1)
#undef TR_STEP
      sc = xs[0] * C2;
    }
    SCHED_FENCE();
    const int tp = 64 * (u < 4 ? u : 4) + lane;
    unsigned wov[16];
    {
      const rsrc_t ro = mk_rsrc(wo_ + (size_t)(h * DH + 16 * s) * DM, u < 5 ? 16 * DM * 2 : 0);
#pragma unroll
      for (int k = 0; k < 16; ++k) wov[k] = __builtin_amdgcn_raw_buffer_load_b32(ro, tp * 4, k * (DM * 2), AUX_NT);
    }
    SCHED_FENCE();
    const float mw = wave_max(sc);
    const float p = __builtin_amdgcn_exp2f(sc - mw);
    const unsigned ppk = cvtpk(p, dppf<XOR1>(p));
    const float lw = wave_sum(__uint_as_float(ppk << 16));
    STAMP(3);
    {
      float a[8];
#pragma unroll
      for (int k = 0; k < 8; ++k) a[k] = 0.f;
#define PV_STEP(J)                                                                        \
  {                                                                                       \
    const unsigned pp = dppu<BCAST0 + 2 * (J)>(ppk);                                      \
    _Pragma("unroll") for (int k = 0; k < 4; ++k) {                                       \
      a[2 * k] = dot2(perm_lo(vv[2 * (J)][k], vv[2 * (J) + 1][k]), pp, a[2 * k]);         \
      a[2 * k + 1] = dot2(perm_hi(vv[2 * (J)][k], vv[2 * (J) + 1][k]), pp, a[2 * k + 1]); \
    }                                                                                     \
  }
      PV_STEP(0) PV_STEP(1) PV_STEP(2) PV_STEP(3) PV_STEP(4) PV_STEP(5) PV_STEP(6) PV_STEP(7)
#undef PV_STEP
      lds_o[u * 64 + 4 * c + r] = rows_reduce8(a);
      if (lane == 0) lds_ml[u] = f32x2{mw, lw};
    }
    __syncthreads();
    STAMP(4);
    if (u < 5) {
      unsigned wl[8], wh[8];
#pragma unroll
      for (int j = 0; j < 8; ++j) {
        wl[j] = perm_lo(wov[2 * j], wov[2 * j + 1]);
        wh[j] = perm_hi(wov[2 * j], wov[2 * j + 1]);
        asm volatile("" : "+v"(wl[j]), "+v"(wh[j]));
      }
      const int p2_off = WS_P2 + (tp >> 2) * (256 * 64) + b * 64 + (tp & 3) * 16;
      while (lds_ld(&lds_ctl[2]) == 0u) {}
      MEM_FENCE();
      STAMP(5);
      const v4u oa = *(const v4u*)&lds_of[0], ob = *(const v4u*)&lds_of[4];
      float y0 = 0.f, y1 = 0.f;
#pragma unroll
      for (int j = 0; j < 4; ++j) {
        y0 = dot2(wl[j], oa[j], y0);
        y1 = dot2(wh[j], oa[j], y1);
      }
#pragma unroll
      for (int j = 0; j < 4; ++j) {
        y0 = dot2(wl[4 + j], ob[j], y0);
        y1 = dot2(wh[4 + j], ob[j], y1);
      }
      st16_sc1(rs, p2_off, y0, y1, tag);
      STAMP(6);
    } else if (u == 5 && b < 80) {
      const int off = WS_P2 + b * (256 * 64) + lane * 16;
      v4u g[16];
      for (unsigned spins = 0;; ++spins) {
        MEM_FENCE();
#pragma unroll
        for (int i = 0; i < 16; ++i) g[i] = ld16_sc1(rs, off + i * 1024);
        bool ok = true;
#pragma unroll
        for (int i = 0; i < 16; ++i) ok &= (g[i].y == tag) & (g[i].w == tag);
        if (__all(ok) || spins > SPIN_CAP) break;
      }
      float y0 = 0.f, y1 = 0.f;
#pragma unroll
      for (int i = 0; i < 16; ++i) {
        y0 += __uint_as_float(g[i].x);
        y1 += __uint_as_float(g[i].z);
      }
      y0 += dppf<ROR4>(y0);
      y1 += dppf<ROR4>(y1);
      y0 += dppf<ROR8>(y0);
      y1 += dppf<ROR8>(y1);
      y0 = xrow_sum(y0);
      y1 = xrow_sum(y1);
      if (lane < 4) ((GAS unsigned*)out_)[b * 4 + lane] = cvtpk(y0, y1);
      if (b == 0 && lane == 0) __builtin_amdgcn_raw_buffer_store_b32(tag, rs, WS_EPOCH, 0, AUX_SC1);
      STAMP(7);
    }
  }
}
}

extern "C" void kernel_launch(void* const* d_in, const int* in_sizes, int n_in, void* d_out, int out_size, void* d_ws, size_t ws_size,
                              hipStream_t stream) {
  (void)in_sizes; (void)n_in; (void)out_size; (void)ws_size;
  fused_decode<<<NH * NSPLIT, 576, 0, stream>>>((const unsigned short*)d_in[0], (const unsigned short*)d_in[1], (const unsigned short*)d_in[2],
                                                (const unsigned short*)d_in[3], (const unsigned short*)d_in[4], (unsigned short*)d_out,
                                                (unsigned char*)d_ws);
}
```

```cpp
#include <hip/hip_runtime.h>
#include <hip/hip_bf16.h>
#include <stdint.h>

#define GAS __attribute__((address_space(1)))
#define LAS __attribute__((address_space(3)))
typedef unsigned v4u __attribute__((ext_vector_type(4)));
typedef unsigned v2u __attribute__((ext_vector_type(2)));
typedef float f32x2 __attribute__((ext_vector_type(2)));
typedef __bf16 bf16x2 __attribute__((ext_vector_type(2)));
typedef __amdgpu_buffer_rsrc_t rsrc_t;

namespace {
constexpr int DM = 640, NH = 32, DH = 128, SK = 16384, WIN = 4096, NSPLIT = 8, NQ = NH * DH;
constexpr int CGW = 32;
constexpr int WS_EPOCH = 0;
constexpr int WS_Q0 = 4096;
constexpr int WS_P1 = WS_Q0 + NH * 2048;
constexpr int WS_P2 = WS_P1 + NH * NSPLIT * 1024;
constexpr int WS_BYTES = WS_P2 + 256 * DM * 4;
constexpr int OOB = 0x40000000;
constexpr unsigned SPIN_CAP = 1u << 16;
constexpr unsigned SENT = 0xffffffffu;
constexpr float C2 = 0.08838834764831845f * 1.4426950408889634f;
constexpr int AUX_NT = 2, AUX_SC1 = 16;
constexpr int PLEAD = 5;
constexpr int PCAP = 210;
constexpr int WCAP = 8;
constexpr int PN1 = 8, PA = 128, PB = 113;
constexpr int WSKEW = 3;
constexpr int QT = 380;
constexpr int QMIN = 70;
constexpr int P2TD = 750;
constexpr int P2WAIT = 16;
constexpr int sched(int n) { return (n <= PN1 ? n * PA : PN1 * PA + (n - PN1) * PB) / 4; }
constexpr int VA = 6, VB = 12;

__device__ __forceinline__ float dot2(unsigned a, unsigned b, float acc) {
  return __builtin_amdgcn_fdot2_f32_bf16(__builtin_bit_cast(bf16x2, a), __builtin_bit_cast(bf16x2, b), acc, false);
}
__device__ __forceinline__ unsigned cvtpk(float lo, float hi) {
  bf16x2 v = {(__bf16)lo, (__bf16)hi};
  return __builtin_bit_cast(unsigned, v);
}
template <int CTRL> __device__ __forceinline__ float dppf(float x) {
  return __builtin_bit_cast(float, __builtin_amdgcn_mov_dpp(__builtin_bit_cast(int, x), CTRL, 0xf, 0xf, true));
}
template <int CTRL> __device__ __forceinline__ unsigned dppu(unsigned x) {
  return (unsigned)__builtin_amdgcn_mov_dpp((int)x, CTRL, 0xf, 0xf, true);
}
constexpr int XOR1 = 0xB1, XOR2 = 0x4E, XOR7 = 0x141, ROR8 = 0x128, ROR4 = 0x124, BCAST0 = 0x150;
__device__ __forceinline__ unsigned perm_lo(unsigned a, unsigned b) { return __builtin_amdgcn_perm(b, a, 0x05040100u); }
__device__ __forceinline__ unsigned perm_hi(unsigned a, unsigned b) { return __builtin_amdgcn_perm(b, a, 0x07060302u); }
__device__ __forceinline__ float swap32_sum(float a, float b) {
  auto t = __builtin_amdgcn_permlane32_swap(__float_as_uint(a), __float_as_uint(b), false, false);
  return __uint_as_float(t[0]) + __uint_as_float(t[1]);
}
__device__ __forceinline__ float swap16_sum(float a, float b) {
  auto t = __builtin_amdgcn_permlane16_swap(__float_as_uint(a), __float_as_uint(b), false, false);
  return __uint_as_float(t[0]) + __uint_as_float(t[1]);
}
__device__ __forceinline__ float xrow_max(float x) {
  auto s = __builtin_amdgcn_permlane16_swap(__float_as_uint(x), __float_as_uint(x), false, false);
  x = fmaxf(__uint_as_float(s[0]), __uint_as_float(s[1]));
  auto t = __builtin_amdgcn_permlane32_swap(__float_as_uint(x), __float_as_uint(x), false, false);
  return fmaxf(__uint_as_float(t[0]), __uint_as_float(t[1]));
}
__device__ __forceinline__ float xrow_sum(float x) {
  x = swap16_sum(x, x);
  return swap32_sum(x, x);
}
__device__ __forceinline__ float wave_max(float x) {
  x = fmaxf(x, dppf<XOR1>(x));
  x = fmaxf(x, dppf<XOR2>(x));
  x = fmaxf(x, dppf<ROR4>(x));
  x = fmaxf(x, dppf<ROR8>(x));
  return xrow_max(x);
}
__device__ __forceinline__ float wave_sum(float x) {
  x += dppf<XOR1>(x);
  x += dppf<XOR2>(x);
  x += dppf<ROR4>(x);
  x += dppf<ROR8>(x);
  return xrow_sum(x);
}
__device__ __forceinline__ f32x2 rows_reduce8(const float (&a)[8]) {
  const float u0 = swap32_sum(a[0], a[4]), u1 = swap32_sum(a[1], a[5]), u2 = swap32_sum(a[2], a[6]), u3 = swap32_sum(a[3], a[7]);
  f32x2 t;
  t.x = swap16_sum(u0, u2);
  t.y = swap16_sum(u1, u3);
  return t;
}
__device__ __forceinline__ rsrc_t mk_rsrc(const void* p, int bytes) { return __builtin_amdgcn_make_buffer_rsrc((void*)p, 0, bytes, 0x00020000); }
__device__ __forceinline__ v4u ld16_sc1(rsrc_t rs, int off) { return __builtin_amdgcn_raw_buffer_load_b128(rs, off, 0, AUX_SC1); }
__device__ __forceinline__ v2u ld8_sc1(rsrc_t rs, int off) { return __builtin_amdgcn_raw_buffer_load_b64(rs, off, 0, AUX_SC1); }
__device__ __forceinline__ unsigned tg(float v, unsigned tag) { return ((__float_as_uint(v) + 0x80u) & 0xffffff00u) | tag; }
__device__ __forceinline__ float utg(unsigned v) { return __uint_as_float(v & 0xffffff00u); }
__device__ __forceinline__ void st8_sc1(rsrc_t rs, int off, float a, float b, unsigned tag) {
  __builtin_amdgcn_raw_buffer_store_b64(v2u{tg(a, tag), tg(b, tag)}, rs, off, 0, AUX_SC1);
}
__device__ __forceinline__ unsigned bad4(v4u g, unsigned tag) { return ((g.x ^ tag) | (g.y ^ tag) | (g.z ^ tag) | (g.w ^ tag)) & 0xffu; }
__device__ __forceinline__ unsigned bad2(v2u g, unsigned tag) { return ((g.x ^ tag) | (g.y ^ tag)) & 0xffu; }
__device__ __forceinline__ unsigned max8(v4u a, v4u b) {
  return max(max(max(a.x, a.y), max(a.z, a.w)), max(max(b.x, b.y), max(b.z, b.w)));
}
#define SCHED_FENCE() __builtin_amdgcn_sched_barrier(0)
#define MEM_FENCE() asm volatile("" ::: "memory")
#define LGKM0() asm volatile("s_waitcnt lgkmcnt(0)" ::: "memory")
template <int N> __device__ __forceinline__ void waitvm() { asm volatile("s_waitcnt vmcnt(%0)" ::"n"(N) : "memory"); }
__device__ __forceinline__ unsigned lds_ld(const unsigned* p) {
  return __builtin_amdgcn_readfirstlane(__hip_atomic_load((const LAS unsigned*)p, __ATOMIC_RELAXED, __HIP_MEMORY_SCOPE_WORKGROUP));
}
__device__ __forceinline__ void lds_st(unsigned* p, unsigned v) { __hip_atomic_store((LAS unsigned*)p, v, __ATOMIC_RELAXED, __HIP_MEMORY_SCOPE_WORKGROUP); }
__device__ __forceinline__ unsigned now() { return (unsigned)__builtin_amdgcn_s_memrealtime(); }
__device__ __forceinline__ void pace_until(unsigned t) {
  while ((int)(now() - t) < 0) __builtin_amdgcn_s_sleep(1);
}

__global__ void __launch_bounds__(576, 3)
fused_decode(const unsigned short* __restrict__ x_, const unsigned short* __restrict__ kc_, const unsigned short* __restrict__ vc_,
             const unsigned short* __restrict__ wq_, const unsigned short* __restrict__ wo_, unsigned short* __restrict__ out_,
             unsigned char* __restrict__ ws_) {
  __shared__ __attribute__((aligned(16))) f32x2 lds_o[8 * 64];
  __shared__ __attribute__((aligned(16))) f32x2 lds_ml[8];
  __shared__ __attribute__((aligned(16))) unsigned lds_y[32 * 8];
  __shared__ __attribute__((aligned(16))) unsigned lds_q[64];
  __shared__ __attribute__((aligned(16))) unsigned lds_of[8];
  __shared__ __attribute__((aligned(16))) unsigned lds_ctl[8];
  const int b = blockIdx.x, h = b & 31, s = b >> 5;
  const int tid = threadIdx.x, lane = tid & 63, w = __builtin_amdgcn_readfirstlane(tid >> 6), r = lane >> 4, c = lane & 15;
  asm volatile("" ::"s"(x_), "s"(kc_), "s"(vc_), "s"(wq_), "s"(wo_), "s"(out_), "s"(ws_));
  const rsrc_t rs = mk_rsrc(ws_, WS_BYTES);

  if (__builtin_expect(w == 0, 1)) {
    __builtin_amdgcn_s_setprio(3);
    const unsigned ts0 = now();
    const int rq = s >> 1, ch = s & 1, g8 = lane >> 3, c8 = lane & 7;
    const rsrc_t rw = mk_rsrc(wq_ + (size_t)(160 * rq) * NQ + h * DH + 64 * ch, 159 * NQ * 2 + 128);
    v4u wqv[20];
#pragma unroll
    for (int i = 0; i < 20; ++i) wqv[i] = __builtin_amdgcn_raw_buffer_load_b128(rw, g8 * (20 * NQ * 2) + c8 * 16, i * (NQ * 2), AUX_NT);
    unsigned xp[10];
    {
      const rsrc_t rx = mk_rsrc(x_, DM * 2);
#pragma unroll
      for (int t = 0; t < 5; ++t) {
        const v2u v = __builtin_amdgcn_raw_buffer_load_b64(rx, g8 * 40 + t * 8, rq * 320, AUX_NT);
        xp[2 * t] = v.x;
        xp[2 * t + 1] = v.y;
      }
    }
    const unsigned E = __builtin_amdgcn_raw_buffer_load_b32(rs, WS_EPOCH, 0, AUX_SC1);
    if (lane < 8) {
      lds_st(&lds_ctl[lane], 0u);
      lds_st(&lds_of[lane], SENT);
    }
    *(v4u*)&lds_y[lane * 4] = v4u{SENT, SENT, SENT, SENT};
    LGKM0();
    SCHED_FENCE();
    __builtin_amdgcn_s_barrier();
    SCHED_FENCE();
    float a[8];
#pragma unroll
    for (int k = 0; k < 8; ++k) a[k] = 0.f;
#pragma unroll
    for (int t = 0; t < 10; ++t) {
#pragma unroll
      for (int k = 0; k < 4; ++k) {
        a[2 * k] = dot2(perm_lo(wqv[2 * t][k], wqv[2 * t + 1][k]), xp[t], a[2 * k]);
        a[2 * k + 1] = dot2(perm_hi(wqv[2 * t][k], wqv[2 * t + 1][k]), xp[t], a[2 * k + 1]);
      }
    }
    f32x2 qp = rows_reduce8(a);
    qp.x += dppf<ROR8>(qp.x);
    qp.y += dppf<ROR8>(qp.y);
    const unsigned E1 = __builtin_amdgcn_readfirstlane(E) + 1u, tag = E1 & 0xffu;
    if (c < 8) st8_sc1(rs, WS_Q0 + h * 2048 + (ch * 4 + rq) * 256 + (4 * c8 + r) * 8, qp.x, qp.y, tag);
    {
      v2u g[4];
      const int off = WS_Q0 + h * 2048 + (lane >> 5) * 1024 + (lane & 31) * 8;
      const unsigned tpub = now();
      pace_until(ts0 + QT);
      pace_until(tpub + QMIN);
      for (unsigned spins = 0;; ++spins) {
        MEM_FENCE();
#pragma unroll
        for (int i = 0; i < 4; ++i) g[i] = ld8_sc1(rs, off + i * 256);
        const unsigned bad = bad2(g[0], tag) | bad2(g[1], tag) | bad2(g[2], tag) | bad2(g[3], tag);
        if (__all(bad == 0u) || spins > SPIN_CAP) break;
      }
      const float q0 = (utg(g[0].x) + utg(g[1].x)) + (utg(g[2].x) + utg(g[3].x));
      const float q1 = (utg(g[0].y) + utg(g[1].y)) + (utg(g[2].y) + utg(g[3].y));
      lds_q[lane] = cvtpk(q0, q1);
      if (lane == 0) lds_st(&lds_ctl[1], E1);
      LGKM0();
      if (lane == 0) lds_st(&lds_ctl[0], 1u);
    }
    const int in_off = WS_P1 + (h * NSPLIT + s) * 1024 + lane * 16;
    {
      const v4u t = ld16_sc1(rs, in_off);
      asm volatile("" ::"v"(t));
    }
    {
      pace_until(ts0 + P2TD);
      const unsigned t = __builtin_amdgcn_raw_buffer_load_b32(rs, lane < DM / CGW ? WS_P2 + lane * (256 * CGW * 4) + b * (CGW * 4) : OOB, 0, AUX_SC1);
      asm volatile("" ::"v"(t));
    }
    float F[8], M, L;
    {
      while (lds_ld(&lds_ctl[3]) < 8u) __builtin_amdgcn_s_sleep(1);
      MEM_FENCE();
      M = lds_ml[0].x;
#pragma unroll
      for (int k = 1; k < 8; ++k) M = fmaxf(M, lds_ml[k].x);
      M = utg(tg(M, 0u));
      L = 0.f;
#pragma unroll
      for (int k = 0; k < 8; ++k) {
        F[k] = __builtin_amdgcn_exp2f(lds_ml[k].x - M);
        L = fmaf(F[k], lds_ml[k].y, L);
      }
      const float inv = 1.0f / L;
#pragma unroll
      for (int k = 0; k < 8; ++k) F[k] *= inv;
    }
    const bool mine = (lane >> 3) == s;
    const int p1_off = mine ? OOB : WS_P1 + ((h * NSPLIT + (lane >> 3)) * NSPLIT + s) * 128 + (lane & 7) * 16;
    const unsigned tM = tg(M, tag), tL = tg(L, tag);
    __syncthreads();
    v4u own;
    {
      float o0 = 0.f, o1 = 0.f, o2 = 0.f, o3 = 0.f;
#pragma unroll
      for (int k = 0; k < 4; ++k) {
        const f32x2 ov = lds_o[k * 64 + lane], ow = lds_o[(k + 4) * 64 + lane];
        o0 = fmaf(F[k], ov.x, o0);
        o1 = fmaf(F[k], ov.y, o1);
        o2 = fmaf(F[k + 4], ow.x, o2);
        o3 = fmaf(F[k + 4], ow.y, o3);
      }
      own = v4u{tg(o0 + o2, tag), tg(o1 + o3, tag), tM, tL};
      __builtin_amdgcn_raw_buffer_store_b128(own, rs, p1_off, 0, AUX_SC1);
    }
    {
      v4u g;
      const int off = mine ? OOB : in_off;
      for (unsigned spins = 0;; ++spins) {
        MEM_FENCE();
        g = ld16_sc1(rs, off);
        if (mine) g = own;
        if (__all(bad4(g, tag) == 0u) || spins > SPIN_CAP) break;
      }
      const float ms = utg(g.z);
      float mx = fmaxf(ms, dppf<ROR8>(ms));
      mx = xrow_max(mx);
      const float e = utg(g.w) * __builtin_amdgcn_exp2f(ms - mx);
      float o0 = e * utg(g.x), o1 = e * utg(g.y), tot = e;
      o0 += dppf<ROR8>(o0);
      o1 += dppf<ROR8>(o1);
      tot += dppf<ROR8>(tot);
      o0 = xrow_sum(o0);
      o1 = xrow_sum(o1);
      tot = xrow_sum(tot);
      const float inv = __builtin_amdgcn_rcpf(tot);
      unsigned opk = cvtpk(o0 * inv, o1 * inv);
      opk = opk == SENT ? 0x7fc07fc0u : opk;
      if (lane < 8) lds_st(&lds_of[lane], opk);
    }
    if (b < DM / CGW) {
      v4u ya, yb;
      for (;;) {
        MEM_FENCE();
        ya = *(volatile LAS v4u*)&lds_y[(lane & 31) * 8];
        yb = *(volatile LAS v4u*)&lds_y[(lane & 31) * 8 + 4];
        if (!__any(max8(ya, yb) == SENT)) break;
      }
      const float t = ((__uint_as_float(ya.x) + __uint_as_float(ya.y)) + (__uint_as_float(ya.z) + __uint_as_float(ya.w))) +
                      ((__uint_as_float(yb.x) + __uint_as_float(yb.y)) + (__uint_as_float(yb.z) + __uint_as_float(yb.w)));
      if (lane < 32) ((GAS unsigned short*)out_)[b * CGW + lane] = (unsigned short)(cvtpk(t, t) & 0xffffu);
      if (b == 0 && lane == 0) __builtin_amdgcn_raw_buffer_store_b32(E1, rs, WS_EPOCH, 0, AUX_SC1);
    }
  } else {
    const int u = w - 1;
    const size_t kvbase = ((size_t)h * SK + (SK - WIN) + (u * NSPLIT + s) * 64) * DH;
    const rsrc_t rk = mk_rsrc(kc_ + kvbase, 64 * DH * 2), rv = mk_rsrc(vc_ + kvbase, 64 * DH * 2);
    const int rot = (b * 5 + (b >> 3)) & 15;
    v4u kv[16], vv[16];
    SCHED_FENCE();
    __builtin_amdgcn_s_barrier();
    SCHED_FENCE();
    unsigned tp0 = now() + u * WSKEW;
#define PACED(I)                                                                                                          \
  {                                                                                                                       \
    if ((I) == PLEAD) {                        \
      waitvm<PLEAD - 1>();                                                                                                \
      const unsigned t1 = now() | 1u;                                                                                     \
      unsigned old = 0;                                                                                                   \
      if (lane == 0)                                                                                                      \
        __hip_atomic_compare_exchange_strong((LAS unsigned*)&lds_ctl[5], &old, t1, __ATOMIC_RELAXED, __ATOMIC_RELAXED,    \
                                             __HIP_MEMORY_SCOPE_WORKGROUP);                                               \
      old = __builtin_amdgcn_readfirstlane(old);                                                                          \
      const unsigned d_ = (old ? old : t1) + u * WSKEW - tp0;                                                             \
      tp0 += d_ > PCAP ? PCAP : d_;                                                                                       \
    }                                                                                                                     \
    if ((I) >= WCAP) waitvm<WCAP - 1>();                                                                                  \
    if ((I) >= PLEAD) pace_until(tp0 + sched((I) - PLEAD));                                                               \
  }
#pragma unroll
    for (int i = 0; i < 16; ++i) {
      PACED(i)
      kv[i] = __builtin_amdgcn_raw_buffer_load_b128(rk, lane * 16, ((i + rot) & 15) * 1024, AUX_NT);
      SCHED_FENCE();
    }
#define V_ISSUE(I)                                                                                   \
  {                                                                                                  \
    PACED(16 + (I))                                                                                  \
    vv[I] = __builtin_amdgcn_raw_buffer_load_b128(rv, lane * 16, (((I) + rot) & 15) * 1024, AUX_NT); \
    SCHED_FENCE();                                                                                   \
  }
    unsigned ppk = 0;
    auto scores = [&]() {
      MEM_FENCE();
      const v4u q = *(const v4u*)&lds_q[4 * c];
      float xs[16];
#pragma unroll
      for (int i = 0; i < 16; ++i) {
        float d = 0.f;
#pragma unroll
        for (int k = 0; k < 4; ++k) d = dot2(kv[i][k], q[k], d);
        xs[i] = d;
      }
#define TR_STEP(HALF, CTRL, BIT)                                          \
  _Pragma("unroll") for (int j = 0; j < (HALF); ++j) {                    \
    const float lo_ = xs[j], hi_ = xs[j + (HALF)];                        \
    const float t1_ = lo_ + dppf<CTRL>(lo_), t2_ = hi_ + dppf<CTRL>(hi_); \
    xs[j] = (c & (BIT)) ? t2_ : t1_;                                      \
  }
      TR_STEP(8, ROR8, 8) TR_STEP(4, XOR7, 4) TR_STEP(2, XOR2, 2) TR_STEP(1, XOR1, 1)
#undef TR_STEP
      const float sc = xs[0] * C2;
      const float mw = wave_max(sc);
      const float p = __builtin_amdgcn_exp2f(sc - mw);
      ppk = cvtpk(p, dppf<XOR1>(p));
      const float lw = wave_sum(__uint_as_float(ppk << 16));
      if (lane == 0) {
        lds_ml[u] = f32x2{mw, lw};
        LGKM0();
        __hip_atomic_fetch_add((LAS unsigned*)&lds_ctl[3], 1u, __ATOMIC_RELAXED, __HIP_MEMORY_SCOPE_WORKGROUP);
      }
      SCHED_FENCE();
    };
#pragma unroll
    for (int i = 0; i < VA; ++i) V_ISSUE(i)
    const bool early = lds_ld(&lds_ctl[0]) != 0u;
    if (early) scores();
#pragma unroll
    for (int i = VA; i < VB; ++i) V_ISSUE(i)
    if (!early) {
      while (lds_ld(&lds_ctl[0]) == 0u) __builtin_amdgcn_s_sleep(2);
      scores();
    }
    const unsigned tag = lds_ld(&lds_ctl[1]) & 0xffu;
    const rsrc_t ro = mk_rsrc(wo_ + (size_t)(h * DH + 16 * s) * DM, u < 5 ? 16 * DM * 2 : 0);
    v4u wov[4];
    {
      float a[8];
#pragma unroll
      for (int k = 0; k < 8; ++k) a[k] = 0.f;
#define PV_STEP(J)                                                                        \
  {                                                                                       \
    const unsigned pp = dppu<BCAST0 + 2 * (J)>(ppk);                                      \
    _Pragma("unroll") for (int k = 0; k < 4; ++k) {                                       \
      a[2 * k] = dot2(perm_lo(vv[2 * (J)][k], vv[2 * (J) + 1][k]), pp, a[2 * k]);         \
      a[2 * k + 1] = dot2(perm_hi(vv[2 * (J)][k], vv[2 * (J) + 1][k]), pp, a[2 * k + 1]); \
    }                                                                                     \
    SCHED_FENCE();                                                                        \
    if (VB + (J) < 16) V_ISSUE((VB + (J)) & 15)                                           \
  }
      PV_STEP(0) PV_STEP(1) PV_STEP(2) PV_STEP(3) PV_STEP(4) PV_STEP(5) PV_STEP(6)
#pragma unroll
      for (int k = 0; k < 4; ++k) wov[k] = __builtin_amdgcn_raw_buffer_load_b128(ro, r * (4 * DM * 2) + u * 256 + c * 16, k * (DM * 2), AUX_NT);
      SCHED_FENCE();
      PV_STEP(7)
#undef PV_STEP
      lds_o[u * 64 + 4 * c + r] = rows_reduce8(a);
    }
    __syncthreads();
    if (u < 5) {
      __builtin_amdgcn_s_setprio(2);
      unsigned W[16];
#pragma unroll
      for (int i = 0; i < 4; ++i) {
        auto s0 = __builtin_amdgcn_permlane32_swap(wov[i][0], wov[i][2], false, false);
        auto s1 = __builtin_amdgcn_permlane32_swap(wov[i][1], wov[i][3], false, false);
        auto ta = __builtin_amdgcn_permlane16_swap(s0[0], s1[0], false, false);
        auto tb = __builtin_amdgcn_permlane16_swap(s0[1], s1[1], false, false);
        W[i] = ta[0];
        W[4 + i] = ta[1];
        W[8 + i] = tb[0];
        W[12 + i] = tb[1];
      }
      unsigned wl[8], wh[8];
#pragma unroll
      for (int j = 0; j < 8; ++j) {
        wl[j] = perm_lo(W[2 * j], W[2 * j + 1]);
        wh[j] = perm_hi(W[2 * j], W[2 * j + 1]);
      }
      const int tp = 64 * u + 4 * c + r;
      int p2_off = WS_P2 + (tp / (CGW / 2)) * (256 * CGW * 4) + b * (CGW * 4) + (tp % (CGW / 2)) * 8;
      asm volatile("" : "+v"(wl[0]), "+v"(wl[1]), "+v"(wl[2]), "+v"(wl[3]), "+v"(wl[4]), "+v"(wl[5]), "+v"(wl[6]), "+v"(wl[7]), "+v"(p2_off));
      asm volatile("" : "+v"(wh[0]), "+v"(wh[1]), "+v"(wh[2]), "+v"(wh[3]), "+v"(wh[4]), "+v"(wh[5]), "+v"(wh[6]), "+v"(wh[7]));
      v4u oa, ob;
      for (;;) {
        MEM_FENCE();
        oa = *(volatile LAS v4u*)&lds_of[0];
        ob = *(volatile LAS v4u*)&lds_of[4];
        if (!__any(max8(oa, ob) == SENT)) break;
      }
      float y0 = 0.f, y1 = 0.f, y2 = 0.f, y3 = 0.f;
#pragma unroll
      for (int j = 0; j < 4; ++j) {
        y0 = dot2(wl[j], oa[j], y0);
        y1 = dot2(wh[j], oa[j], y1);
        y2 = dot2(wl[4 + j], ob[j], y2);
        y3 = dot2(wh[4 + j], ob[j], y3);
      }
      st8_sc1(rs, p2_off, y0 + y2, y1 + y3, tag);
    }
    if (b < DM / CGW) {
      const int off = WS_P2 + b * (256 * CGW * 4) + u * (32 * CGW * 4) + lane * 16;
      v4u g[4];
      while (lds_ld(&lds_of[7]) == SENT) __builtin_amdgcn_s_sleep(4);
      __builtin_amdgcn_s_sleep(P2WAIT);
      for (unsigned spins = 0;; ++spins) {
        MEM_FENCE();
#pragma unroll
        for (int i = 0; i < 4; ++i) g[i] = ld16_sc1(rs, off + i * 1024);
        const unsigned bad = bad4(g[0], tag) | bad4(g[1], tag) | bad4(g[2], tag) | bad4(g[3], tag);
        if (__all(bad == 0u) || spins > SPIN_CAP) break;
        __builtin_amdgcn_s_sleep(1);
      }
      float y[4];
#pragma unroll
      for (int k = 0; k < 4; ++k) y[k] = (utg(g[0][k]) + utg(g[1][k])) + (utg(g[2][k]) + utg(g[3][k]));
      float t = swap16_sum(swap32_sum(y[0], y[2]), swap32_sum(y[1], y[3]));
      t += dppf<ROR8>(t);
      unsigned tb = __float_as_uint(t);
      tb = tb == SENT ? 0x7fc00000u : tb;
      if (c < 8) lds_st(&lds_y[(4 * c + r) * 8 + u], tb);
    }
  }
}
}

extern "C" void kernel_launch(void* const* d_in, const int* in_sizes, int n_in, void* d_out, int out_size, void* d_ws, size_t ws_size,
                              hipStream_t stream) {
  (void)in_sizes; (void)n_in; (void)out_size; (void)ws_size;
  fused_decode<<<NH * NSPLIT, 576, 0, stream>>>((const unsigned short*)d_in[0], (const unsigned short*)d_in[1], (const unsigned short*)d_in[2],
                                                (const unsigned short*)d_in[3], (const unsigned short*)d_in[4], (unsigned short*)d_out,
                                                (unsigned char*)d_ws);
}
```
